```python
import torch

def _softmax(x, axis=-1):
    return torch.softmax(x, dim=axis)
def _gelu(x):
    return torch.nn.functional.gelu(x, approximate="tanh")
def _silu(x):
    return torch.nn.functional.silu(x)
def _sigmoid(x):
    return torch.sigmoid(x)
def _layernorm(x, axis=-1, eps=1e-5):
    m = x.mean(dim=axis, keepdim=True)
    v = x.var(dim=axis, keepdim=True, unbiased=False)
    return (x - m) / torch.sqrt(v + eps)
def _scatter_add(tgt, idx, src, axis=0):
    return tgt.clone().index_add_(axis, idx, src)
def _segment_sum(src, seg, n):
    out = torch.zeros((n,) + src.shape[1:], dtype=src.dtype)
    return out.index_add_(0, seg, src)
def _pad(x, pw):
    flat = tuple(v for pair in reversed(pw) for v in pair)
    return torch.nn.functional.pad(x, flat)

kernel_name = 'standalone__detection_seg_fpn_lateral__vision'

def setup_inputs():

    _w = (lambda a: (lambda t: t.to(torch.bfloat16) if t.is_floating_point() else t)(torch.as_tensor(a)))
    H = W = (224 // 2) * 2
    C = 192
    k = 3
    return {
        "c_fine": _w((lambda *s: torch.randn(*s, dtype=torch.bfloat16) * 0.05)(1, H, W, C)),
        "p_coarse": _w((lambda *s: torch.randn(*s, dtype=torch.bfloat16) * 0.05)(1, H // 2, W // 2, C)),
        "w_lat": _w((lambda *s: torch.randn(*s, dtype=torch.bfloat16) * 0.05)(C, C)),
        "w_out": _w((lambda *s: torch.randn(*s, dtype=torch.bfloat16) * 0.05)(k, k, C, C)),
    }

def reference(c_fine, p_coarse, w_lat, w_out):
    B, H, W, C = c_fine.shape
    k = 3
    lat = torch.einsum("bhwc,cd->bhwd", c_fine, w_lat)
    up = (lambda x, n, axis: torch.repeat_interleave(x, n, dim=axis))((lambda x, n, axis: torch.repeat_interleave(x, n, dim=axis))(p_coarse, 2, axis=1), 2, axis=2)
    m = lat + up
    pad = k // 2
    mp = _pad(m, ((0, 0), (pad, pad), (pad, pad), (0, 0)))
    o = None
    for di in range(k):
        for dj in range(k):
            t = torch.einsum("bhwc,cd->bhwd", mp[:, di:di + H, dj:dj + W, :], w_out[di, dj])
            o = t if o is None else o + t
    return o
```

```cpp
#include <hip/hip_runtime.h>
#include <hip/hip_bf16.h>
#include <utility>

typedef __bf16 bf16x8 __attribute__((ext_vector_type(8)));
typedef __bf16 bf16x4 __attribute__((ext_vector_type(4)));
typedef __bf16 bf16x2 __attribute__((ext_vector_type(2)));
typedef float f32x4 __attribute__((ext_vector_type(4)));
typedef float f32x2 __attribute__((ext_vector_type(2)));
typedef unsigned u32x4 __attribute__((ext_vector_type(4)));
typedef unsigned u32x2 __attribute__((ext_vector_type(2)));
typedef unsigned long long u64;
#define LDS_AS __attribute__((address_space(3)))
#define DI __device__ __forceinline__
#define GAP() __builtin_amdgcn_sched_barrier(0)

constexpr int IMG = 224, CIMG = 112, CH = 192, TS = 14;
constexpr int PIXB = CH * 2;
constexpr int ROWB = 16 * PIXB;
constexpr int SLAB = 32 * PIXB;
constexpr int PREG = 20 * ROWB;
constexpr int SCR = 26 * ROWB;
constexpr int LDS_BYTES = SCR + 3072 + 256;
constexpr int OROW = TS * PIXB;
constexpr int OST1 = 13 * ROWB, OST0 = OST1 + 7 * OROW;
constexpr unsigned OOB = 0x40000000u;
constexpr unsigned LDS_OOB = 0x100000u;
constexpr int NKS = 72;
constexpr int NFR = 7 * NKS;
constexpr int PFN = 9;
constexpr int U_BYTES = NKS * SLAB;
constexpr int FSTR = 8;
constexpr int WS_FLAGS = 4096, WS_U = WS_FLAGS + 4096;
constexpr int SC1 = 16;
constexpr unsigned SPIN_CAP = 1024;
constexpr int RAMP = 46;
constexpr int u_issued(int n) { return n < RAMP ? 3 * (n + 6) : (3 * (RAMP + 5) + 4 * (n - RAMP + 1) < 3 * NKS ? 3 * (RAMP + 5) + 4 * (n - RAMP + 1) : 3 * NKS); }
constexpr int v0_slot(int ty) { return ty < 5 ? 16 + ty : 9 + ty; }
constexpr int wl_slab(int k) { return k < 5 ? 10 * ROWB + k * SLAB : 24 * ROWB; }
static_assert(OST0 + 7 * OROW <= SCR, "staging fits");

template <class F, int... I>
DI void static_for_impl(F&& f, std::integer_sequence<int, I...>) { (f(std::integral_constant<int, I>{}), ...); }
template <int N, class F>
DI void static_for(F&& f) { static_for_impl(f, std::make_integer_sequence<int, N>{}); }

DI unsigned pack2(float lo, float hi) {
  f32x2 v = {lo, hi};
  return __builtin_bit_cast(unsigned, __builtin_convertvector(v, bf16x2));
}
DI float bf_lo(unsigned a) { return __builtin_bit_cast(float, a << 16); }
DI float bf_hi(unsigned a) { return __builtin_bit_cast(float, a & 0xffff0000u); }
DI unsigned opaque(unsigned x) { asm volatile("" : "+v"(x)); return x; }
DI float opaque_f(float x) { asm volatile("" : "+v"(x)); return x; }
DI bf16x4 tr_read(unsigned addr) {
  return __builtin_amdgcn_ds_read_tr16_b64_v4bf16((LDS_AS bf16x4*)(size_t)addr);
}
DI bf16x8 cat8(bf16x4 lo, bf16x4 hi) {
  return __builtin_shufflevector(lo, hi, 0, 1, 2, 3, 4, 5, 6, 7);
}
DI bf16x8 lds_read16(unsigned addr) { return *(LDS_AS bf16x8*)(size_t)addr; }
DI u32x4 lds_read16u(unsigned addr) { return *(LDS_AS u32x4*)(size_t)addr; }
DI u32x2 lds_read8u(unsigned addr) { return *(LDS_AS u32x2*)(size_t)addr; }
DI void put16(unsigned addr, const unsigned* v) { u32x4 x = {v[0], v[1], v[2], v[3]}; *(LDS_AS u32x4*)(size_t)addr = x; }
DI void put8(unsigned addr, const unsigned* v) { u32x2 x = {v[4], v[5]}; *(LDS_AS u32x2*)(size_t)addr = x; }

DI void mfma_acc(f32x4& d, bf16x8 a, bf16x8 b) {
  asm volatile("v_mfma_f32_16x16x32_bf16 %0, %1, %2, %0" : "+v"(d) : "a"(a), "a"(b));
}
DI void mfma_init(f32x4& d, bf16x8 a, bf16x8 b, f32x4 c) {
  asm volatile("v_mfma_f32_16x16x32_bf16 %0, %1, %2, %3" : "=&v"(d) : "a"(a), "a"(b), "v"(c));
}
DI void mfma_acc_a(f32x4& d, u32x4 a, bf16x8 b) {
  asm volatile("v_mfma_f32_16x16x32_bf16 %0, %1, %2, %0" : "+a"(d) : "a"(a), "a"(b));
}
DI void mfma_acc_p(f32x4& d, u32x4 a, bf16x8 b) {
  asm volatile("v_mfma_f32_16x16x32_bf16 %0, %1, %2, %0" : "+v"(d) : "a"(a), "v"(b));
}
DI void mfma_zero_a(f32x4& d, u32x4 a, bf16x8 b) {
  asm volatile("v_mfma_f32_16x16x32_bf16 %0, %1, %2, 0" : "=&a"(d) : "a"(a), "a"(b));
}

template <int OFF, bool NT>
DI void dma_piece(unsigned m0v, unsigned voff, __amdgpu_buffer_rsrc_t srd, unsigned soff) {
  if constexpr (NT)
    asm volatile("s_mov_b32 m0, %0\n\ts_nop 0\n\tbuffer_load_dwordx4 %1, %2, %3 offen offset:%c4 nt lds"
                 :: "s"(m0v), "v"(voff), "s"(srd), "s"(soff), "i"(OFF) : "memory");
  else
    asm volatile("s_mov_b32 m0, %0\n\ts_nop 0\n\tbuffer_load_dwordx4 %1, %2, %3 offen offset:%c4 lds"
                 :: "s"(m0v), "v"(voff), "s"(srd), "s"(soff), "i"(OFF) : "memory");
}
DI void dma_slab(unsigned ldsdst, unsigned voff, __amdgpu_buffer_rsrc_t srd, unsigned soff) {
  dma_piece<0, false>(ldsdst, voff, srd, soff);
  dma_piece<128, false>(ldsdst + 896, voff, srd, soff);
  dma_piece<256, false>(ldsdst + 1792, voff, srd, soff);
}
#define WAIT_BAR(str) do { GAP(); asm volatile("s_waitcnt " str "\n\ts_barrier" ::: "memory"); GAP(); } while (0)
#define WAIT_BAR2(str0, str) do { GAP(); if (w == 0) asm volatile("s_waitcnt " str0 ::: "memory"); \
  else asm volatile("s_waitcnt " str ::: "memory"); asm volatile("s_barrier" ::: "memory"); GAP(); } while (0)
#define BAR() do { GAP(); asm volatile("s_barrier" ::: "memory"); GAP(); } while (0)
constexpr int wait_vm(int n) { return (n & 15) | (7 << 4) | (15 << 8) | ((n >> 4) << 14); }
constexpr int wait_lgkm(int n) { return 15 | (7 << 4) | (n << 8) | (3 << 14); }

DI void make_unit(u32x4* u, u32x4 g0, u32x4 g1, u32x4 g2) {
#pragma unroll
  for (int j = 0; j < 4; ++j) {
    const float s0 = bf_lo(g0[j]) + bf_lo(g2[j]), s1 = bf_hi(g0[j]) + bf_hi(g2[j]);
    const float c0 = bf_lo(g1[j]), c1 = bf_hi(g1[j]);
    u[0][j] = pack2(0.5f * (s0 - c0), 0.5f * (s1 - c1));
    u[1][j] = pack2(0.5f * (s0 + c0), 0.5f * (s1 + c1));
    u[3][j] = g2[j] ^ 0x80008000u;
  }
  u[2] = g0;
}
DI void store_unit(__amdgpu_buffer_rsrc_t srdU, unsigned voff, unsigned slab_off, const u32x4* u) {
#pragma unroll
  for (int p = 0; p < 4; ++p) __builtin_amdgcn_raw_buffer_store_b128(u[p], srdU, voff, slab_off + 18 * p * SLAB, SC1);
}
DI unsigned frag_place(int n, int q) {
  const int ws = n / 48, c = n - 48 * ws;
  const int t = c < 32 ? (c >> 2) & 1 : 2, i = c < 32 ? 4 * (c >> 3) + (c & 3) : c - 32;
  return (unsigned)((3 * ws + t) * 1024 + (q * 16 + i) * 16);
}

constexpr int NT = 66, FT = NT * 7;
constexpr int frag_n(int f) { return f < FT ? f / 7 : NT + (f - FT) % 6; }
constexpr int frag_j(int f) { return f < FT ? f % 7 : (f - FT) / 6; }

__global__ __launch_bounds__(256, 1)
void fpn_fused_kernel(const void* __restrict__ cfine, const void* __restrict__ pcoarse,
                      const void* __restrict__ wlat, const void* __restrict__ wout,
                      void* __restrict__ out, void* ws) {
  __shared__ __attribute__((aligned(128))) char smem[LDS_BYTES];
  const unsigned lds0 = (unsigned)(size_t)(LDS_AS char*)smem;
  asm volatile("" :: "s"(cfine), "s"(pcoarse), "s"(wlat), "s"(wout), "s"(out), "s"(ws));
  u64 epoch;
  asm volatile("s_load_dwordx2 %0, %1, 0x0" : "=s"(epoch) : "s"(ws) : "memory");
  const int lane = threadIdx.x & 63;
  const int w = __builtin_amdgcn_readfirstlane(threadIdx.x >> 6);
  const int col = lane & 15, q = lane >> 4;

  const int b = blockIdx.x, grp = b & 7, idx = b >> 3;
  const int ty0 = (grp >> 1) * 4 + (idx >> 3), tx0 = (grp & 1) * 8 + (idx & 7);

  const __amdgpu_buffer_rsrc_t srdC = __builtin_amdgcn_make_buffer_rsrc((void*)cfine, (short)0, IMG * IMG * PIXB, 0x00020000);
  const __amdgpu_buffer_rsrc_t srdP = __builtin_amdgcn_make_buffer_rsrc((void*)pcoarse, (short)0, CIMG * CIMG * PIXB, 0x00020000);
  const __amdgpu_buffer_rsrc_t srdL = __builtin_amdgcn_make_buffer_rsrc((void*)wlat, (short)0, 6 * SLAB, 0x00020000);
  const __amdgpu_buffer_rsrc_t srdW = __builtin_amdgcn_make_buffer_rsrc((void*)wout, (short)0, 54 * SLAB, 0x00020000);
  const __amdgpu_buffer_rsrc_t srdF = __builtin_amdgcn_make_buffer_rsrc(ws, (short)0, WS_U, 0x00020000);
  const __amdgpu_buffer_rsrc_t srdU = __builtin_amdgcn_make_buffer_rsrc((char*)ws + WS_U, (short)0, U_BYTES, 0x00020000);
  const __amdgpu_buffer_rsrc_t srdO = __builtin_amdgcn_make_buffer_rsrc(out, (short)0, IMG * IMG * PIXB, 0x00020000);

  const bool producer = b < 216;
  const int un = b / 12, uT = b - 12 * un;
  if (w == 0) {
    const unsigned src = (unsigned)((16 * (lane >> 5) + 8 * ((lane >> 3) & 1) + 4 * ((lane >> 4) & 1) + ((lane >> 1) & 3)) * PIXB
                                    + (lane & 1) * 16);
    const unsigned so = producer ? (unsigned)(un * SLAB + uT * 32) : OOB;
    dma_piece<0, false>(lds0 + SCR, src, srdW, so);
    dma_piece<0, false>(lds0 + SCR + 1024, src, srdW, so + 18 * SLAB);
    dma_piece<0, false>(lds0 + SCR + 2048, src, srdW, so + 36 * SLAB);
  }
  if (w == 3) {
    u64 pc; unsigned len;
    asm volatile("s_getpc_b64 %0\n.Lfpn_pc:\n\ts_mov_b32 %1, .Lfpn_end-.Lfpn_pc\n\ts_max_u32 %1, %1, .Lfpn_slow-.Lfpn_pc"
                 : "=s"(pc), "=s"(len) :: "scc");
    const __amdgpu_buffer_rsrc_t srdI = __builtin_amdgcn_make_buffer_rsrc((void*)pc, (short)0, len, 0x00020000);
    asm volatile("s_mov_b32 m0, %0\n\ts_nop 0\n\tbuffer_load_dword %1, %2, %3 offen lds"
                 :: "s"(lds0 + SCR + 3072), "v"(lane * 128), "s"(srdI), "s"((idx & 7) * 8192) : "memory");
  }

  const int hh = w & 1, rpar = w >> 1;
  unsigned csrc;
  {
    const int X = TS * tx0 - 1 + 8 * hh + (lane >> 3);
    csrc = (unsigned)X < (unsigned)IMG ? (unsigned)(X * PIXB + (((lane ^ (lane >> 3)) & 7) * 16)) : OOB;
  }
  auto issue_c = [&](int i, int kp) {
    const int yy = 2 * i + rpar, Y = TS * ty0 - 1 + yy;
    const unsigned soff = (unsigned)Y < (unsigned)IMG ? (unsigned)(Y * IMG * PIXB) : OOB;
    const unsigned dst = lds0 + yy * ROWB + hh * 1024 + kp * 1920;
    if (kp == 0) dma_piece<0, true>(dst, csrc, srdC, soff);
    if (kp == 1) dma_piece<128, true>(dst, csrc, srdC, soff);
    if (kp == 2) dma_piece<256, true>(dst, csrc, srdC, soff);
  };
  auto issue_crow = [&](int i) { issue_c(i, 0); issue_c(i, 1); issue_c(i, 2); };
  unsigned psrc;
  {
    const int L = 64 * w + lane, px = L / 28, cs = L - 28 * px, c = cs ^ (px >> 2);
    const int CX = 7 * tx0 - 1 + px;
    psrc = (px < 9 && cs < 24 && (unsigned)CX < (unsigned)CIMG) ? (unsigned)(CX * PIXB + c * 16) : OOB;
  }
  auto issue_prow = [&](int r) {
    const int CY = 7 * ty0 - 1 + r;
    const unsigned soff = (unsigned)CY < (unsigned)CIMG ? (unsigned)(CY * CIMG * PIXB) : OOB;
    dma_piece<0, true>(lds0 + PREG + r * 4096 + w * 1024, psrc, srdP, soff);
  };

  issue_crow(0); issue_prow(0); issue_prow(1); issue_crow(1);
  const unsigned wsrc = (unsigned)((16 * (w >> 1) + 8 * ((lane >> 4) & 1) + 4 * (w & 1) + ((lane >> 2) & 3)) * PIXB
                                   + (lane >> 5) * 64 + (((lane & 3) ^ ((lane >> 3) & 2)) * 16));
#pragma unroll
  for (int k = 0; k < 1; ++k) dma_slab(lds0 + wl_slab(k) + w * 3072, wsrc, srdL, k * SLAB);
  GAP();

  const unsigned cr0 = lds0 + col * 128 + ((q ^ (col & 7)) * 16), cr1 = cr0 ^ 64;
  const unsigned cr0h = opaque(cr0 + 10 * ROWB), cr1h = opaque(cr1 + 10 * ROWB);
  auto frag_addr = [&](int slot, int k) {
    return slot < 10 ? ((k & 1) ? cr1 : cr0) + slot * ROWB + (k >> 1) * 2048
                     : ((k & 1) ? cr1h : cr0h) + (slot - 10) * ROWB + (k >> 1) * 2048;
  };
  const int chA = 6 * w + q;
  const int chB = 6 * w + 4 + (q >> 1);
  unsigned prdA, prdB;
  {
    const int cx = (col + 1) >> 1;
    prdA = opaque(lds0 + PREG + cx * 448 + ((chA ^ (cx >> 2)) * 16));
    prdB = opaque(lds0 + PREG + cx * 448 + ((chB ^ (cx >> 2)) * 16) + (q & 1) * 8);
  }
  const unsigned mwA = lds0 + (chA >> 3) * 2048 + col * 128 + (((chA ^ col) & 7) * 16);
  const unsigned mwC = lds0 + (chB >> 3) * 2048 + col * 128 + (((chB ^ col) & 7) * 16) + (q & 1) * 8;
  const unsigned mwAh = opaque(mwA + 10 * ROWB), mwCh = opaque(mwC + 10 * ROWB);
  auto wA = [&](int slot) { return slot < 10 ? mwA + slot * ROWB : mwAh + (slot - 10) * ROWB; };
  auto wC = [&](int slot) { return slot < 10 ? mwC + slot * ROWB : mwCh + (slot - 10) * ROWB; };
  unsigned wla[3], wlb[3];
#pragma unroll
  for (int t = 0; t < 3; ++t) {
    const int p = lane & 3, o = 96 * w + (t < 2 ? 16 * p + 8 * t : 64 + 8 * p);
    const unsigned a = lds0 + (lane >> 5) * 6144 + (o >> 6) * 512 + ((lane >> 2) & 7) * 64
                       + ((((o >> 4) & 3) ^ ((q & 1) * 2)) * 16) + (o & 8);
    wla[t] = opaque(a + wl_slab(0));
    wlb[t] = opaque(a + wl_slab(5));
  }
  unsigned va[3][2][2];
#pragma unroll
  for (int dj = 0; dj < 3; ++dj) {
    const int x = col + dj;
    const unsigned a = col < TS ? lds0 + x * 128 + ((q ^ (x & 7)) * 16) : LDS_OOB;
    va[dj][0][0] = opaque(a);
    va[dj][1][0] = opaque(a ^ 64);
    va[dj][0][1] = opaque(a + 10 * ROWB);
    va[dj][1][1] = opaque((a ^ 64) + 10 * ROWB);
  }
  const unsigned obase = (unsigned)((TS * ty0 * IMG + TS * tx0) * PIXB);
  unsigned oA, oC, sl[4], sg[4];
  auto piece_addr = [&](int i) {
    int ty = 0, rem = (int)threadIdx.x;
    bool valid = true;
    if (i > 0) { const int m = (i - 1) * 256 + (int)threadIdx.x; ty = m / 80; rem = 256 + m - 80 * ty; valid = m < 560; }
    const int x = rem / 24, c = rem - 24 * x;
    sl[i] = opaque(lds0 + OST1 + ty * OROW + x * PIXB + (((c & ~7) | ((c ^ x) & 7)) * 16));
    sg[i] = opaque(valid ? obase + (unsigned)(2 * ty * IMG * PIXB + rem * 16) : OOB);
  };
#pragma unroll
  for (int i = 0; i < 4; ++i) piece_addr(i);
  oA = opaque(col < TS ? lds0 + OST1 + col * PIXB + (((chA & ~7) | ((chA ^ col) & 7)) * 16) : LDS_OOB);
  oC = opaque(col < TS ? lds0 + OST1 + col * PIXB + (((chB & ~7) | ((chB ^ col) & 7)) * 16) + (q & 1) * 8 : LDS_OOB);
  const unsigned uplace = opaque(producer ? frag_place(16 * uT + col, q) : OOB);
  GAP();
#pragma unroll
  for (int k = 1; k < 6; ++k) dma_slab(lds0 + wl_slab(k) + w * 3072, wsrc, srdL, k * SLAB);
  GAP();
  if (w == 0) {
    asm volatile("s_waitcnt vmcnt(26) lgkmcnt(0)" ::: "memory");
    const unsigned a = lds0 + SCR + (lane >> 5) * 512 + (lane & 31) * 8;
    u32x4 g[3], un4[4];
#pragma unroll
    for (int di = 0; di < 3; ++di)
      g[di] = __builtin_bit_cast(u32x4, cat8(tr_read(a + di * 1024), tr_read(a + di * 1024 + 256)));
    make_unit(un4, g[0], g[1], g[2]);
    store_unit(srdU, uplace, (unsigned)(un * SLAB), un4);
  }
  GAP();
  issue_crow(2); issue_prow(2);
  WAIT_BAR2("vmcnt(8) lgkmcnt(0)", "vmcnt(4) lgkmcnt(0)");
  asm volatile("" : "+s"(epoch));
  const u64 tag = epoch + 1;
  const u32x2 tag2 = {(unsigned)tag, (unsigned)(tag >> 32)};

  bf16x8 wlf[6][3], cf[2][6];
  auto read_wl = [&](int k, int t) {
    const unsigned a = k < 5 ? wla[t] + k * SLAB : wlb[t];
    wlf[k][t] = cat8(tr_read(a), tr_read(a + 3072));
  };
  f32x4 up[9][3], D[16][3];
  u32x4 prawA[9]; u32x2 prawB[9];
  auto unpack_pair = [&](int prow, int u) {
    const unsigned x = u < 4 ? prawA[prow][u] : prawB[prow][u - 4];
    up[prow][u >> 1][2 * (u & 1)] = opaque_f(bf_lo(x));
    up[prow][u >> 1][2 * (u & 1) + 1] = opaque_f(bf_hi(x));
  };
  prawA[0] = lds_read16u(prdA);
  prawB[0] = lds_read8u(prdB);
  GAP();
#pragma unroll
  for (int k = 0; k < 2; ++k) {
    cf[0][k] = lds_read16(frag_addr(0, k));
#pragma unroll
    for (int t = 0; t < 3; ++t) read_wl(k, t);
    GAP();
  }
  prawA[1] = lds_read16u(prdA + 4096);
  prawB[1] = lds_read8u(prdB + 4096);
  GAP();
  issue_crow(3); issue_prow(3);
  GAP();
#pragma unroll
  for (int u = 0; u < 6; ++u) unpack_pair(0, u);
  GAP();
  asm volatile("s_nop 1");

  const unsigned uoff = (unsigned)(w * 3072 + lane * 16);
  u32x4 wr[NKS][3];
  auto load_u = [&](int n, int t) {
    wr[n][t] = __builtin_amdgcn_raw_buffer_load_b128(srdU, uoff + t * 1024, n * SLAB, SC1);
  };
  u32x4 fl[2][2];
  const unsigned foff = w == 0 ? (unsigned)(WS_FLAGS + lane * 16) : OOB;
  auto flags_ok = [&](const u32x4* f) {
    unsigned d = 0;
#pragma unroll
    for (int i = 0; i < 2; ++i) d |= (f[i][0] ^ tag2[0]) | (f[i][1] ^ tag2[1]) | (f[i][2] ^ tag2[0]) | (f[i][3] ^ tag2[1]);
    return __builtin_amdgcn_ballot_w64(d != 0) == 0;
  };
  bool all_seen = false;
  u32x2 enow = {0, 0};

  unsigned vt[6];
  float x0 = 0.f, x1 = 0.f;
  auto pair_sub = [&](const f32x4* x, const f32x4* y, int h) {
    x0 = opaque_f(x[h >> 1][2 * (h & 1)] - y[h >> 1][2 * (h & 1)]);
    x1 = opaque_f(x[h >> 1][2 * (h & 1) + 1] - y[h >> 1][2 * (h & 1) + 1]);
  };
  auto pair_add = [&](const f32x4* x, const f32x4* y, int h) {
    x0 = opaque_f(x[h >> 1][2 * (h & 1)] + y[h >> 1][2 * (h & 1)]);
    x1 = opaque_f(x[h >> 1][2 * (h & 1) + 1] + y[h >> 1][2 * (h & 1) + 1]);
  };
  auto v_addr = [&](int n, int j) {
    const int pi = n / 18, dj = (n / 6) % 3, k = n % 6;
    const int slot = pi == 2 ? v0_slot(j) : (pi == 1 ? 2 * j + 1 : 2 * j);
    if (slot >= 10) return va[dj][k & 1][1] + (slot - 10) * ROWB + (k >> 1) * 2048;
    return va[dj][k & 1][0] + slot * ROWB + (k >> 1) * 2048;
  };
  bf16x8 pf[PFN];

  static_for<16>([&](auto rc) __attribute__((always_inline)) {
    constexpr int r = decltype(rc)::value, i = r >> 1;
    constexpr bool even = (r & 1) == 0;
#pragma unroll
    for (int g = 0; g < 18; ++g) {
      const int k = g / 3, t = g % 3;
      if (k == 0) mfma_init(D[r][t], wlf[0][t], cf[r & 1][0], up[(r + 1) >> 1][t]);
      else mfma_acc(D[r][t], wlf[k][t], cf[r & 1][k]);
      GAP();
      if (g == 5) asm volatile("" :: "v"(up[(r + 1) >> 1][0]), "v"(up[(r + 1) >> 1][1]), "v"(up[(r + 1) >> 1][2]));
      if (t == 2 && r < 14) cf[r & 1][k] = lds_read16(frag_addr(r + 2, k));
      if constexpr (!even && i >= 1) {
        if ((t == 1 && k >= 1) || g == 17) { GAP(); vt[g == 17 ? 5 : k - 1] = opaque(pack2(x0, x1)); }
        if ((t == 0 && k >= 1) || g == 16) pair_sub(D[r - 1], D[r - 2], g == 16 ? 5 : k - 1);
        if (g == 14) put16(wA(2 * (i - 1)), vt);
        if (g == 17) put8(wC(2 * (i - 1)), vt);
      }
      if constexpr (r == 0) {
        if (g < 12) read_wl(k + 2, t);
        if (t == 0 && k < 4) cf[0][k + 2] = lds_read16(frag_addr(0, k + 2));
        if (g >= 12) cf[1][g - 12] = lds_read16(frag_addr(1, g - 12));
        if (t == 1) unpack_pair(1, k);
      } else if constexpr (even) {
        if (g == 1) prawA[i + 1] = lds_read16u(prdA + (i + 1) * 4096);
        if (g == 3) prawB[i + 1] = lds_read8u(prdB + (i + 1) * 4096);
        if (g >= 7 && g < 16 && t != 2) unpack_pair(i + 1, (g - 7) - (g - 6) / 3);
      }
      if constexpr (r >= 1 && r <= 4) {
        if (g == 0) issue_c(r + 3, 0);
        if (g == 4) issue_c(r + 3, 1);
        if (g == 8) issue_c(r + 3, 2);
        if (g == 12) issue_prow(r + 3);
        if (g == 16 && r == 4) issue_prow(8);
      }
      if constexpr (r >= 12) {
        const int u = (r - 12) * 18 + g;
        if (u % 5 == 0) load_u(u / 15, (u / 5) % 3);
      }
      if constexpr (r == 15) { if (t == 2 && k < 4) pf[k] = lds_read16(v_addr(0, k)); }
      GAP();
    }
    if constexpr (r == 0) {
      asm volatile("s_waitcnt vmcnt(8)" ::: "memory");
      __builtin_amdgcn_raw_buffer_store_b64(tag2, srdF, threadIdx.x == 0 ? (unsigned)(WS_FLAGS + b * FSTR) : OOB, 0, SC1);
      GAP();
    }
    if constexpr (r == 1) WAIT_BAR("vmcnt(9)");
    if constexpr (r == 3) WAIT_BAR("vmcnt(13)");
    if constexpr (r == 5) WAIT_BAR("vmcnt(13)");
    if constexpr (r == 7) WAIT_BAR("vmcnt(9)");
    if constexpr (r == 8 || r == 10) {
      u32x4* f = fl[r == 10];
      asm volatile("buffer_load_dwordx4 %0, %2, %3, 0 offen sc1\n\tbuffer_load_dwordx4 %1, %2, %3, 0 offen offset:1024 sc1"
                   : "=&v"(f[0]), "=&v"(f[1]) : "v"(foff), "s"(srdF) : "memory");
      GAP();
    }
    if constexpr (r == 9) WAIT_BAR("vmcnt(7)");
    if constexpr (r == 11) {
      GAP();
      asm volatile("s_waitcnt vmcnt(2)" : "+v"(fl[0][0]), "+v"(fl[0][1]) :: "memory");
      if (w == 0) {
        bool ok = flags_ok(fl[0]);
        if (__builtin_expect(!ok, 0)) {
          asm volatile("s_waitcnt vmcnt(0)" : "+v"(fl[1][0]), "+v"(fl[1][1]) :: "memory");
          ok = flags_ok(fl[1]);
          for (unsigned spins = 0; spins < SPIN_CAP && !ok; ++spins) {
            u32x4 f[2];
            f[0] = __builtin_amdgcn_raw_buffer_load_b128(srdF, foff, 0, SC1);
            f[1] = __builtin_amdgcn_raw_buffer_load_b128(srdF, foff + 1024, 0, SC1);
            ok = flags_ok(f);
            if (!ok) __builtin_amdgcn_s_sleep(8);
          }
          asm volatile(".Lfpn_slow:");
          if (!ok) {
            for (int u = 0; u < 216; ++u) {
              const int sn = u / 12, n = 16 * (u - 12 * sn) + col;
              u32x4 g[3], uf[4];
#pragma unroll
              for (int di = 0; di < 3; ++di)
#pragma unroll
                for (int j = 0; j < 4; ++j) {
                  const unsigned o = (unsigned)((di * 18 + sn) * SLAB + (8 * q + 2 * j) * PIXB + n * 2);
                  const unsigned lo = __builtin_amdgcn_raw_buffer_load_b16(srdW, o, 0, 0);
                  const unsigned hi = __builtin_amdgcn_raw_buffer_load_b16(srdW, o + PIXB, 0, 0);
                  g[di][j] = (lo & 0xffffu) | (hi << 16);
                }
              make_unit(uf, g[0], g[1], g[2]);
              store_unit(srdU, frag_place(n, q), (unsigned)(sn * SLAB), uf);
            }
            asm volatile("s_waitcnt vmcnt(0)" ::: "memory");
          }
        }
        all_seen = ok;
      }
      WAIT_BAR("lgkmcnt(8)");
    }
    if constexpr (r == 15) {
      asm volatile("s_waitcnt vmcnt(15)" : "+v"(fl[1][0]), "+v"(fl[1][1]) :: "memory");
      WAIT_BAR("lgkmcnt(0)");
    }
  });

  f32x4 accB[7][3], accS[7][3], accZ[7][3], accP[7][3];
  auto piece_read = [&](int i, int odd) {
    constexpr int other = OST0 - OST1;
    return i < 7 ? lds_read16u(sl[0] + i * OROW + (odd ? other : 0)) : lds_read16u(sl[i - 6] + (odd ? other : 0));
  };
  auto piece_store = [&](u32x4 v, int i, int odd) {
    if (i < 7) __builtin_amdgcn_raw_buffer_store_b128(v, srdO, sg[0], (2 * i + odd) * IMG * PIXB, SC1);
    else __builtin_amdgcn_raw_buffer_store_b128(v, srdO, sg[i - 6], odd * IMG * PIXB, SC1);
  };
  u32x4 sv, sw;
  float e0, e1;

#pragma unroll
  for (int f = 4; f < PFN; ++f) pf[f] = lds_read16(v_addr(frag_n(f), frag_j(f)));
  GAP();
  static_for<NFR>([&](auto fc) __attribute__((always_inline)) {
    constexpr int f = decltype(fc)::value, n = frag_n(f), j = frag_j(f);
    constexpr int pi = n / 18;
    constexpr bool first = (n % 18) == 0;
#pragma unroll
    for (int s = 0; s < 3; ++s) {
      const int t = (j & 1) ? 2 - s : s;
      const bf16x8 a = __builtin_bit_cast(bf16x8, wr[n][t]);
      if constexpr (n < 15) {
        if (first) mfma_zero_a(accB[j][t], wr[n][t], pf[f % PFN]); else mfma_acc_a(accB[j][t], wr[n][t], pf[f % PFN]);
      } else if constexpr (pi == 0) {
        accB[j][t] = __builtin_amdgcn_mfma_f32_16x16x32_bf16(a, pf[f % PFN], accB[j][t], 0, 0, 0);
      } else if constexpr (pi == 1) {
        accS[j][t] = __builtin_amdgcn_mfma_f32_16x16x32_bf16(a, pf[f % PFN], first ? accB[j][t] : accS[j][t], 0, 0, 0);
      } else if constexpr (pi == 2) {
        accZ[j][t] = __builtin_amdgcn_mfma_f32_16x16x32_bf16(a, pf[f % PFN], first ? accS[j][t] : accZ[j][t], 0, 0, 0);
      } else {
        mfma_acc_p(accP[j][t], wr[n][t], pf[f % PFN]);
      }
      GAP();
      if constexpr (f >= 1 && f + PFN - 1 < NFR) {
        if (s == 0) pf[(f - 1) % PFN] = lds_read16(v_addr(frag_n(f + PFN - 1), frag_j(f + PFN - 1)));
      }
      if constexpr (f % 3 == 2 && f >= 8 && f + PFN < NFR) {
        if (s == 2) __builtin_amdgcn_s_waitcnt(wait_lgkm(PFN - 4));
      }
      if constexpr (f < FT) {
        {
          constexpr int l0 = u_issued(n - 1), cnt = u_issued(n) - l0;
          constexpr int x = cnt <= 3 ? ((j & 1) ? j >> 1 : 7) : (j == 0 ? 0 : j == 2 ? 1 : j == 3 ? 2 : j == 5 ? 3 : 7);
          if constexpr (x < cnt) { if (s == 1) load_u((l0 + x) / 3, (l0 + x) % 3); }
          if (s == 1 && j == 6 && n < 57) __builtin_amdgcn_s_waitcnt(wait_vm(u_issued(n) - 3 * (n + 2)));
        }
        {
          constexpr int u = f < 126 ? f - 4 : f - 126 + 98;
          constexpr bool act = (f >= 4 && f < 4 + 98) || (f >= 126 && f < 126 + 49);
          if constexpr (act) {
            constexpr int kind = u / 49, ty = (u % 49) / 7, ph = u % 7;
            constexpr int slot = kind == 0 ? 2 * ty + 1 : (kind == 1 ? v0_slot(ty) : 2 * ty);
            if constexpr (ph >= 1) { if (s == 0) { GAP(); vt[ph - 1] = opaque(pack2(x0, x1)); } }
            if constexpr (ph < 6) {
              if (s == 2) {
                if constexpr (kind == 0) pair_add(D[2 * ty + 1], D[2 * ty + 2], ph);
                if constexpr (kind == 1) pair_sub(D[2 * ty], D[2 * ty + 2], ph);
                if constexpr (kind == 2) pair_sub(D[2 * ty + 1], D[2 * ty + 3], ph);
              }
            }
            if (ph == 6 && s == 2) put8(wC(slot), vt);
            if (ph == 5 && s == 1 && !(j & 1)) put16(wA(slot), vt);
            if (ph == 6 && s == 1 && ((j + 6) % 7 & 1)) put16(wA(slot), vt);
          }
        }
        if constexpr (n >= 37 && n < 49) {
          constexpr int u = f - 37 * 7, jy = u / 12, e = u % 12;
          if (s == 0) e0 = opaque_f(accB[jy][e >> 2][e & 3]);
          if (s == 1) e1 = opaque_f(accS[jy][e >> 2][e & 3]);
          if (s == 2) accP[jy][e >> 2][e & 3] = opaque_f(__builtin_fmaf(-2.f, e0, e1));
        }
        if constexpr (n >= 54 && n < 61) {
          constexpr int jy = n - 54;
          if constexpr (j < 6) {
            if (s == 0) e0 = opaque_f(accZ[jy][j >> 1][2 * (j & 1)]);
            if (s == 1) e1 = opaque_f(accZ[jy][j >> 1][2 * (j & 1) + 1]);
            if (s == 2) { GAP(); vt[j] = opaque(pack2(e0, e1)); }
          }
          if (j == 4 && s == 1) put16(oA + jy * OROW, vt);
          if (j == 6 && s == 1) put8(oC + jy * OROW, vt);
        }
      } else if constexpr (j >= 1) {
        constexpr int jy = j - 1;
        const int ph = 3 * (n - NT) + s, h = ph - 4;
        if (h >= 0 && h < 6) vt[h] = opaque(pack2(accP[jy][h >> 1][2 * (h & 1)], accP[jy][h >> 1][2 * (h & 1) + 1]));
        if (ph == 10) put16(oA + (OST0 - OST1) + jy * OROW, vt);
        if (ph == 11) put8(oC + (OST0 - OST1) + jy * OROW, vt);
      }
      if constexpr (f < FT && n >= 56 && n < 66) {
        constexpr int order[10] = {0, 1, 2, 3, 7, 4, 5, 6, 8, 9};
        if (j == 2 && s == 2) sv = piece_read(order[n - 56], 0);
        if (j == 6 && s == 2) piece_store(sv, order[n - 56], 0);
      }
      if constexpr (f >= FT && j >= 2) {
        if (n == NT && s == 1) sv = piece_read(j - 2, 1);
        if (n == NT + 2 && s == 2) piece_store(sv, j - 2, 1);
        if constexpr (j == 5) {
          if (n == NT + 3 && s == 0) sw = piece_read(7, 1);
          if (n == NT + 5 && s == 1) piece_store(sw, 7, 1);
        }
      }
      GAP();
    }
    if constexpr (f < FT && j == 1 && (n == 56 || n == 58 || n == 61)) WAIT_BAR("lgkmcnt(2)");
    if constexpr (f >= FT && n == NT + 5 && j >= 1 && j <= 4) WAIT_BAR("lgkmcnt(2)");
    if constexpr (f >= FT && n == NT + 5 && j == 5) WAIT_BAR("lgkmcnt(0)");
    if constexpr (f < FT && j == 6) {
      if constexpr (n == 15 || n == 36) WAIT_BAR("lgkmcnt(7)");
      if constexpr (n == 17 || n == 53) BAR();
      if constexpr (n == 5) {
        if (all_seen && b == 0) __builtin_amdgcn_raw_buffer_store_b64(tag2, srdF, lane == 0 ? 0u : OOB, 0, SC1);
        GAP();
      }
      if constexpr (n == 56) { enow = __builtin_amdgcn_raw_buffer_load_b64(srdF, 0, 0, SC1); GAP(); }
    }
  });

  asm volatile("s_nop 7");
  GAP();
#pragma unroll
  for (int h = 0; h < 6; ++h) vt[h] = pack2(accP[6][h >> 1][2 * (h & 1)], accP[6][h >> 1][2 * (h & 1) + 1]);
  put16(oA + (OST0 - OST1) + 6 * OROW, vt);
  put8(oC + (OST0 - OST1) + 6 * OROW, vt);
  WAIT_BAR("lgkmcnt(0)");
  {
    constexpr int rest[4] = {5, 6, 8, 9};
    u32x4 v[4];
#pragma unroll
    for (int i = 0; i < 4; ++i) v[i] = piece_read(rest[i], 1);
#pragma unroll
    for (int i = 0; i < 4; ++i) piece_store(v[i], rest[i], 1);
  }
  if (all_seen && __builtin_amdgcn_readfirstlane((enow[0] ^ tag2[0]) | (enow[1] ^ tag2[1])) != 0)
    __builtin_amdgcn_raw_buffer_store_b64(tag2, srdF, lane == 0 ? 0u : OOB, 0, SC1);
  asm volatile(".Lfpn_end:");
}

extern "C" void kernel_launch(void* const* d_in, const int* in_sizes, int n_in,
                              void* d_out, int out_size, void* d_ws, size_t ws_size,
                              hipStream_t stream) {
  (void)in_sizes; (void)n_in; (void)out_size; (void)ws_size;
  fpn_fused_kernel<<<256, 256, 0, stream>>>(d_in[0], d_in[1], d_in[2], d_in[3], d_out, d_ws);
}
```
